```python
import torch

def _softmax(x, axis=-1):
    return torch.softmax(x, dim=axis)
def _gelu(x):
    return torch.nn.functional.gelu(x, approximate="tanh")
def _silu(x):
    return torch.nn.functional.silu(x)
def _sigmoid(x):
    return torch.sigmoid(x)
def _layernorm(x, axis=-1, eps=1e-5):
    m = x.mean(dim=axis, keepdim=True)
    v = x.var(dim=axis, keepdim=True, unbiased=False)
    return (x - m) / torch.sqrt(v + eps)
def _scatter_add(tgt, idx, src, axis=0):
    return tgt.clone().index_add_(axis, idx, src)
def _segment_sum(src, seg, n):
    out = torch.zeros((n,) + src.shape[1:], dtype=src.dtype)
    return out.index_add_(0, seg, src)
def _pad(x, pw):
    flat = tuple(v for pair in reversed(pw) for v in pair)
    return torch.nn.functional.pad(x, flat)

kernel_name = 'interleave_layers__linear_attn_block__sliding_block__prompt'

def setup_inputs():

    _w = (lambda a: (lambda t: t.to(torch.bfloat16) if t.is_floating_point() else t)(torch.as_tensor(a)))
    def _A_setup():
        B, S, D = 1, 2048, 1024
        return {
            "x": _w((lambda *s: torch.randn(*s, dtype=torch.bfloat16) * 0.05)(B, S, D)),
            "wqkv": _w((lambda *s: torch.randn(*s, dtype=torch.bfloat16) * 0.05)(D, 3 * D)),
            "wo": _w((lambda *s: torch.randn(*s, dtype=torch.bfloat16) * 0.05)(D, D)),
        }
    _A_in = _A_setup()

    def _B_setup():
        B, S, D = 1, 2048, 1024
        return {
            "x": _w((lambda *s: torch.randn(*s, dtype=torch.bfloat16) * 0.05)(B, S, D)),
            "wqkv": _w((lambda *s: torch.randn(*s, dtype=torch.bfloat16) * 0.05)(D, 3 * D)),
            "wo": _w((lambda *s: torch.randn(*s, dtype=torch.bfloat16) * 0.05)(D, D)),
        }
    _B_in = _B_setup()

    return {
        "A_x": _A_in["x"],
        "A_wqkv": _A_in["wqkv"],
        "A_wo": _A_in["wo"],
        "B_x": _B_in["x"],
        "B_wqkv": _B_in["wqkv"],
        "B_wo": _B_in["wo"],
    }

def reference(A_x, A_wqkv, A_wo, B_x, B_wqkv, B_wo):
    def _A_ref(x, wqkv, wo):
        B, S, D = x.shape
        H = 32; Dh = D // H
        qkv = (lambda x, *d: x.permute(*d))((x @ wqkv).reshape(B, S, 3, H, Dh), 2, 0, 3, 1, 4)
        q, k, v = qkv[0], qkv[1], qkv[2]
        phi = lambda t: torch.where(t > 0, t + 1, torch.exp(t)) + 1e-6
        pq, pk = phi(q), phi(k)
        kv = torch.einsum("bhsd,bhse->bhde", pk, v)
        z = torch.einsum("bhsd->bhd", pk)
        num = torch.einsum("bhqd,bhde->bhqe", pq, kv)
        den = torch.einsum("bhqd,bhd->bhq", pq, z)[..., None] + 1e-6
        o = num / den
        return (lambda x, *d: x.permute(*d))(o, 0, 2, 1, 3).reshape(B, S, D) @ wo

    def _B_ref(x, wqkv, wo):
        B, S, D = x.shape
        H = 32; Dh = D // H; W = 256
        qkv = (lambda x, *d: x.permute(*d))((x @ wqkv).reshape(B, S, 3, H, Dh), 2, 0, 3, 1, 4)
        q, k, v = qkv[0], qkv[1], qkv[2]
        scores = torch.einsum("bhqd,bhkd->bhqk", q, k) * (Dh ** -0.5)
        idx = torch.arange(S)
        mask = (idx[:, None] - idx[None, :] >= 0) & (idx[:, None] - idx[None, :] < W)
        scores = torch.where(mask[None, None], scores, -1e9)
        p = _softmax(scores, axis=-1)
        o = torch.einsum("bhqk,bhkd->bhqd", p, v)
        return (lambda x, *d: x.permute(*d))(o, 0, 2, 1, 3).reshape(B, S, D) @ wo

    x = A_x
    x = x + _A_ref(_layernorm(x, axis=-1), A_wqkv, A_wo)
    x = x + _B_ref(_layernorm(x, axis=-1), B_wqkv, B_wo)
    x = x + _A_ref(_layernorm(x, axis=-1), A_wqkv, A_wo)
    x = x + _B_ref(_layernorm(x, axis=-1), B_wqkv, B_wo)
    return x
```

```cpp
#include <hip/hip_runtime.h>
#include <hip/hip_bf16.h>
#include <stdint.h>

typedef __attribute__((ext_vector_type(8))) short bf16x8;
typedef __attribute__((ext_vector_type(4))) short bf16x4;
typedef __attribute__((ext_vector_type(4))) float f32x4;
typedef __attribute__((ext_vector_type(2))) float f32x2;
typedef __attribute__((ext_vector_type(4))) unsigned u32x4;
typedef __attribute__((ext_vector_type(2))) unsigned u32x2;
typedef __attribute__((ext_vector_type(2))) __bf16 bf16x2_t;
typedef unsigned short u16;
#define LDS_AS __attribute__((address_space(3)))

constexpr int SEQ = 2048, DM = 1024, NH = 32;

__device__ __forceinline__ float bf2f(u16 v) { return __uint_as_float(((unsigned)v) << 16); }
__device__ __forceinline__ u16 f2bf(float f) {
  __hip_bfloat16 h = __float2bfloat16(f);
  return *reinterpret_cast<u16*>(&h);
}
__device__ __forceinline__ float rbf(float f) { return bf2f(f2bf(f)); }
__device__ __forceinline__ unsigned cvt_pk_bf16(float lo, float hi) {
  f32x2 v = {lo, hi};
  return __builtin_bit_cast(unsigned, __builtin_convertvector(v, bf16x2_t));
}
__device__ __forceinline__ bf16x4 pack4(float a, float b, float c, float d) {
  u32x2 r = {cvt_pk_bf16(a, b), cvt_pk_bf16(c, d)};
  return __builtin_bit_cast(bf16x4, r);
}
__device__ __forceinline__ float phi_f(float t) {
  float r = t > 0.f ? rbf(t + 1.f) : rbf(__expf(t));
  return r + 1e-6f;
}

#define WAIT_V(n) asm volatile("s_waitcnt vmcnt(" #n ")" ::: "memory")
#define WAIT_L(n) asm volatile("s_waitcnt lgkmcnt(" #n ")" ::: "memory")
#define SCHED() __builtin_amdgcn_sched_barrier(0)
#define BAR() __builtin_amdgcn_s_barrier()
#define DS_B128(dst, addr, off) asm volatile("ds_read_b128 %0, %1 offset:%2" : "=v"(dst) : "v"(addr), "i"(off))
#define DS_TR64(dst, addr, off) asm volatile("ds_read_b64_tr_b16 %0, %1 offset:%2" : "=v"(dst) : "v"(addr), "i"(off))

__device__ __forceinline__ void st16(void* p, u32x4 v) {
  asm volatile("global_store_dwordx4 %0, %1, off sc1\n\ts_nop 1" :: "v"(p), "v"(v) : "memory");
}
__device__ __forceinline__ void st8(void* p, u32x2 v) {
  asm volatile("global_store_dwordx2 %0, %1, off sc1\n\ts_nop 1" :: "v"(p), "v"(v) : "memory");
}
__device__ __forceinline__ u32x4 ld16(__amdgpu_buffer_rsrc_t r, unsigned byte_off) {
  return __builtin_amdgcn_raw_buffer_load_b128(r, byte_off, 0, 16);
}
__device__ __forceinline__ __amdgpu_buffer_rsrc_t mk_rsrc(const void* p, unsigned bytes) {
  return __builtin_amdgcn_make_buffer_rsrc(const_cast<void*>(p), 0, bytes, 0x00020000);
}
__device__ __forceinline__ void signal_add(unsigned* p) {
  asm volatile("s_waitcnt vmcnt(0)" ::: "memory");
  __syncthreads();
  if (threadIdx.x == 0) __hip_atomic_fetch_add(p, 1u, __ATOMIC_RELAXED, __HIP_MEMORY_SCOPE_AGENT);
}
__device__ __forceinline__ void poll_ge(unsigned* p, unsigned target) {
  unsigned spins = 0;
  while (__hip_atomic_load(p, __ATOMIC_RELAXED, __HIP_MEMORY_SCOPE_AGENT) < target) {
    __builtin_amdgcn_s_sleep(1);
    if (++spins > (1u << 20)) break;
  }
}

constexpr int CNT_STRIDE = 32;
constexpr size_t MBy = 1u << 20;
__device__ __host__ constexpr size_t off_x(int l) { return (size_t)l * 4 * MBy; }
__device__ __host__ constexpr size_t off_q(int l) { return (16 + (size_t)l * 16) * MBy; }
__device__ __host__ constexpr size_t off_k(int l) { return (20 + (size_t)l * 16) * MBy; }
__device__ __host__ constexpr size_t off_v(int l) { return (24 + (size_t)l * 16) * MBy; }
__device__ __host__ constexpr size_t off_o(int l) { return (28 + (size_t)l * 16) * MBy; }
__device__ __host__ constexpr size_t off_stat(int l) { return (80 + (size_t)l) * MBy; }
__device__ __host__ constexpr size_t off_kv(int l) { return (86 + (size_t)l * 3) * MBy; }
constexpr size_t OFF_COLSUM = 100 * MBy, OFF_CNT = 101 * MBy, OFF_SINK = 102 * MBy;
__device__ __host__ constexpr int cidx_x(int l, int rb) { return ((l * 16 + rb)) * CNT_STRIDE; }
__device__ __host__ constexpr int cidx_o(int l, int rb) { return (64 + (l * 16 + rb)) * CNT_STRIDE; }
__device__ __host__ constexpr int cidx_kv(int l, int ct) { return (128 + (l * 16 + ct)) * CNT_STRIDE; }
__device__ __host__ constexpr int cidx_q(int l, int rb, int ct) { return (192 + ((l * 16 + rb) * 16 + ct)) * CNT_STRIDE; }
constexpr int CNT_WORDS = (192 + 1024) * CNT_STRIDE;

__global__ __launch_bounds__(256) void prep_kernel(const u16* __restrict__ WA, const u16* __restrict__ WB,
                                                   const u16* __restrict__ X, float* __restrict__ colsum,
                                                   float* __restrict__ statp, const u16* __restrict__ WoA,
                                                   const u16* __restrict__ WoB, float* __restrict__ sink,
                                                   unsigned* __restrict__ cnt) {
  __shared__ float red[32][65];
  const int b = blockIdx.x, t = threadIdx.x;
  if (b >= 96 + 512) {
    const int bb = b - (96 + 512);
    for (int i = bb * 256 + t; i < CNT_WORDS; i += 64 * 256) cnt[i] = 0u;
    const u16* W = (bb < 32 ? WoA : WoB) + (size_t)(bb & 31) * 32768 + t * 8;
    unsigned acc = 0;
#pragma unroll
    for (int i = 0; i < 16; ++i) {
      u32x4 v = *reinterpret_cast<const u32x4*>(W + i * 2048);
      acc ^= v[0] ^ v[1] ^ v[2] ^ v[3];
    }
    if (acc == 0x12345678u) sink[bb * 256 + t] = 1.f;
  } else if (b < 96) {
    const int mat = b / 48, cg = b % 48;
    const u16* W = mat ? WB : WA;
    const int c8 = t & 7, rl = t >> 3;
    float s[8];
#pragma unroll
    for (int j = 0; j < 8; ++j) s[j] = 0.f;
#pragma unroll 8
    for (int i = 0; i < 32; ++i) {
      const int k = i * 32 + rl;
      bf16x8 v = *reinterpret_cast<const bf16x8*>(W + (size_t)k * 3072 + cg * 64 + c8 * 8);
#pragma unroll
      for (int j = 0; j < 8; ++j) s[j] += bf2f((u16)v[j]);
    }
#pragma unroll
    for (int j = 0; j < 8; ++j) red[rl][c8 * 8 + j] = s[j];
    __syncthreads();
    if (t < 64) {
      float a = 0.f;
#pragma unroll
      for (int r = 0; r < 32; ++r) a += red[r][t];
      colsum[(size_t)mat * 3072 + cg * 64 + t] = a;
    }
  } else {
    const int row = (b - 96) * 4 + (t >> 6), lane = t & 63;
    const u16* xr = X + (size_t)row * DM + lane * 16;
    bf16x8 v0 = *reinterpret_cast<const bf16x8*>(xr);
    bf16x8 v1 = *reinterpret_cast<const bf16x8*>(xr + 8);
    float s = 0.f, q = 0.f;
#pragma unroll
    for (int j = 0; j < 8; ++j) {
      float a = bf2f((u16)v0[j]), c = bf2f((u16)v1[j]);
      s += a + c; q += a * a + c * c;
    }
    s += __shfl_xor(s, 1); q += __shfl_xor(q, 1);
    s += __shfl_xor(s, 2); q += __shfl_xor(q, 2);
    if ((lane & 3) == 0) {
      f32x2 o; o[0] = s; o[1] = q;
      *reinterpret_cast<f32x2*>(statp + ((size_t)row * 16 + (lane >> 2)) * 2) = o;
    }
  }
}

constexpr int QKV_STAGE = 16384 + 24576;
constexpr int SMEM_BYTES = 4 * QKV_STAGE;

template <int MODE>
__device__ __forceinline__ void qkv_phase(char* smem, const int rb, const int ct, const u16* __restrict__ X,
                                          const u16* __restrict__ W, const float* __restrict__ colsum,
                                          const float* __restrict__ statp, u16* __restrict__ O0, u16* __restrict__ O1,
                                          u16* __restrict__ O2, float* __restrict__ kvpart, unsigned* wait_cnt) {
  int tid = threadIdx.x;
  asm volatile("" : "+v"(tid));
  const int lane = tid & 63, wid = tid >> 6;
  const int wr = wid >> 2, wc = wid & 3;
  const int g = lane >> 4, l15 = lane & 15;
  const int row0 = rb * 128;
  float* s_stat = reinterpret_cast<float*>(smem + 3 * QKV_STAGE);
  float* s_cs = reinterpret_cast<float*>(smem + 3 * QKV_STAGE + 1024);

  const u16* srcA0; const u16* srcA1; const u16* srcB;
  {
    const int r0 = (2 * wid) * 8 + (lane >> 3), r1 = r0 + 8;
    const int c = lane & 7;
    srcA0 = X + (size_t)(row0 + r0) * DM + ((c ^ (r0 & 7)) * 8);
    srcA1 = X + (size_t)(row0 + r1) * DM + ((c ^ (r1 & 7)) * 8);
    const int kb = wid;
    const int rowp = l15 >> 1, half = lane & 1, nb4 = lane >> 4;
    const int k = kb * 8 + (rowp ^ ((kb & 1) * 4));
    srcB = W + (size_t)k * 3072 + ct * 64 + nb4 * 16 + half * 8;
  }
  auto stage_piece = [&](int kt, int buf, const int i) {
    char* base = smem + buf * QKV_STAGE;
    if (i == 0) __builtin_amdgcn_global_load_lds((const unsigned*)(srcA0 + kt * 64), (LDS_AS unsigned*)(base + (2 * wid) * 1024), 16, 0, 16);
    else if (i == 1) __builtin_amdgcn_global_load_lds((const unsigned*)(srcA1 + kt * 64), (LDS_AS unsigned*)(base + (2 * wid + 1) * 1024), 16, 0, 16);
    else __builtin_amdgcn_global_load_lds((const unsigned*)(srcB + (size_t)kt * 64 * 3072 + (i - 2) * 1024),
                                          (LDS_AS unsigned*)(base + 16384 + wid * 3072 + (i - 2) * 1024), 16, 0, 0);
  };
  auto stage = [&](int kt, int buf) {
#pragma unroll
    for (int i = 0; i < 5; ++i) stage_piece(kt, buf, i);
  };

#pragma unroll
  for (int s2 = 0; s2 < 4; ++s2) {
#pragma unroll
    for (int i = 2; i < 5; ++i) stage_piece(s2, s2, i);
  }
  if (wait_cnt) {
    if (threadIdx.x == 0) poll_ge(wait_cnt, 16u);
    asm volatile("s_waitcnt lgkmcnt(0)" ::: "memory");
    BAR();
  }
  f32x4 sp0, sp1; float csv_;
  {
    const float* sp = statp + (size_t)(row0 + (tid >> 2)) * 32 + (tid & 3) * 8;
    const int c = tid < 192 ? tid : 0;
    const float* cp = colsum + (c >> 6) * 1024 + ct * 64 + (c & 63);
    asm volatile("global_load_dwordx4 %0, %3, off sc1\n\tglobal_load_dwordx4 %1, %3, off offset:16 sc1\n\tglobal_load_dword %2, %4, off"
                 : "=&v"(sp0), "=&v"(sp1), "=&v"(csv_) : "v"(sp), "v"(cp) : "memory");
  }
#pragma unroll
  for (int s2 = 0; s2 < 4; ++s2) {
    stage_piece(s2, s2, 0);
    stage_piece(s2, s2, 1);
  }

  f32x4 acc[4][3];
#pragma unroll
  for (int m = 0; m < 4; ++m)
#pragma unroll
    for (int n = 0; n < 3; ++n) acc[m][n] = (f32x4){0.f, 0.f, 0.f, 0.f};

  const int r7 = l15 & 7;
  const int q4 = l15 >> 2, p4 = lane & 3;
  const unsigned sbase = (unsigned)(size_t)smem;
  const unsigned aoff0 = sbase + (wr * 64 + l15) * 128 + ((g ^ r7) * 16);
  const unsigned aoff1 = sbase + (wr * 64 + l15) * 128 + (((4 + g) ^ r7) * 16);
  const int sw = (g & 1) * 4;
  const unsigned boff0 = sbase + 16384 + g * 3072 + wc * 256 + p4 * 8 + ((q4 ^ sw) * 32);
  const unsigned boff1 = sbase + 16384 + g * 3072 + wc * 256 + p4 * 8 + (((4 + q4) ^ sw) * 32);

  constexpr int NT = DM / 64;
  bf16x8 af[2][2][4];
  bf16x4 blo[2][2][3], bhi[2][2][3];
  auto read_all = [&](int kt, const int S) {
    const unsigned bo = (kt & 3) * QKV_STAGE;
    const unsigned a0 = aoff0 + bo, a1 = aoff1 + bo, b0 = boff0 + bo, b1 = boff1 + bo;
    SCHED();
    DS_TR64(blo[S][0][0], b0, 0);     DS_TR64(bhi[S][0][0], b1, 0);
    DS_B128(af[S][0][0], a0, 0);      DS_B128(af[S][0][1], a0, 2048);
    DS_TR64(blo[S][0][1], b0, 1024);  DS_TR64(bhi[S][0][1], b1, 1024);
    DS_B128(af[S][0][2], a0, 4096);   DS_B128(af[S][0][3], a0, 6144);
    DS_TR64(blo[S][0][2], b0, 2048);  DS_TR64(bhi[S][0][2], b1, 2048);
    DS_TR64(blo[S][1][0], b0, 12288); DS_TR64(bhi[S][1][0], b1, 12288);
    DS_B128(af[S][1][0], a1, 0);      DS_B128(af[S][1][1], a1, 2048);
    DS_TR64(blo[S][1][1], b0, 13312); DS_TR64(bhi[S][1][1], b1, 13312);
    DS_B128(af[S][1][2], a1, 4096);   DS_B128(af[S][1][3], a1, 6144);
    DS_TR64(blo[S][1][2], b0, 14336); DS_TR64(bhi[S][1][2], b1, 14336);
    SCHED();
  };
  auto mma = [&](const int S, int ktn, bool do_stage) {
#pragma unroll
    for (int ks = 0; ks < 2; ++ks) {
      SCHED();
      bf16x8 bfr[3];
#pragma unroll
      for (int n = 0; n < 3; ++n) bfr[n] = __builtin_shufflevector(blo[S][ks][n], bhi[S][ks][n], 0, 1, 2, 3, 4, 5, 6, 7);
#pragma unroll
      for (int m = 0; m < 4; ++m) {
        acc[m][0] = __builtin_amdgcn_mfma_f32_16x16x32_bf16(bfr[0], af[S][ks][m], acc[m][0], 0, 0, 0);
        if (MODE == 1)
          acc[m][1] = __builtin_amdgcn_mfma_f32_16x16x32_bf16(bfr[1], af[S][ks][m], acc[m][1], 0, 0, 0);
        else
          acc[m][1] = __builtin_amdgcn_mfma_f32_16x16x32_bf16(af[S][ks][m], bfr[1], acc[m][1], 0, 0, 0);
        acc[m][2] = __builtin_amdgcn_mfma_f32_16x16x32_bf16(af[S][ks][m], bfr[2], acc[m][2], 0, 0, 0);
        SCHED();
        if (do_stage) {
          if (ks == 0 && m < 3) stage_piece(ktn, ktn & 3, m);
          if (ks == 1 && m < 2) stage_piece(ktn, ktn & 3, 3 + m);
        }
        SCHED();
      }
    }
  };
  auto step = [&](int kt, const int S) {
    if (kt == 0) { WAIT_V(4); } else if (kt == 1) { WAIT_V(7); }
    else if (kt + 3 < NT) { WAIT_V(10); } else if (kt + 2 < NT) { WAIT_V(5); } else { WAIT_V(0); }
    WAIT_L(0);
    BAR();
    if (kt + 1 < NT) read_all(kt + 1, S ^ 1);
    mma(S, kt + 4, kt + 4 < NT);
  };
  WAIT_V(6);
  BAR();
  read_all(0, 0);
#pragma unroll
  for (int kt = 0; kt < NT; kt += 2) {
    step(kt, 0);
    step(kt + 1, 1);
  }
  BAR();
  asm volatile("s_waitcnt vmcnt(0)" : "+v"(sp0), "+v"(sp1), "+v"(csv_) :: "memory");
  {
    float s = (sp0[0] + sp0[2]) + (sp1[0] + sp1[2]), q = (sp0[1] + sp0[3]) + (sp1[1] + sp1[3]);
    s += __shfl_xor(s, 1); q += __shfl_xor(q, 1);
    s += __shfl_xor(s, 2); q += __shfl_xor(q, 2);
    const float mean = s * (1.f / 1024.f);
    float var = q * (1.f / 1024.f) - mean * mean;
    var = var < 0.f ? 0.f : var;
    if ((tid & 3) == 0) { s_stat[(tid >> 2) * 2] = mean; s_stat[(tid >> 2) * 2 + 1] = rsqrtf(var + 1e-5f); }
    if (tid < 192) s_cs[tid] = csv_;
  }
  __syncthreads();

  constexpr int QS = 144;
  constexpr int TS = 272;
  char* sQ = smem;
  if (MODE == 1) {
    char* sKk = smem + 128 * QS;
    char* sVv = smem + 256 * QS;
#pragma unroll
    for (int m = 0; m < 4; ++m) {
      const int rl = wr * 64 + m * 16 + l15;
      const float mean = s_stat[rl * 2], rstd = s_stat[rl * 2 + 1];
      {
        const float* cs = s_cs + wc * 16 + g * 4;
        *reinterpret_cast<bf16x4*>(sQ + rl * QS + (wc * 16 + g * 4) * 2) =
            pack4(rstd * (acc[m][0][0] - mean * cs[0]), rstd * (acc[m][0][1] - mean * cs[1]),
                  rstd * (acc[m][0][2] - mean * cs[2]), rstd * (acc[m][0][3] - mean * cs[3]));
      }
      {
        const float* cs = s_cs + 64 + wc * 16 + g * 4;
        *reinterpret_cast<bf16x4*>(sKk + rl * QS + (wc * 16 + g * 4) * 2) =
            pack4(rstd * (acc[m][1][0] - mean * cs[0]), rstd * (acc[m][1][1] - mean * cs[1]),
                  rstd * (acc[m][1][2] - mean * cs[2]), rstd * (acc[m][1][3] - mean * cs[3]));
      }
      {
        const int rv = wr * 64 + m * 16 + g * 4;
        const float cs = s_cs[128 + wc * 16 + l15];
        float vv[4];
#pragma unroll
        for (int j = 0; j < 4; ++j)
          vv[j] = s_stat[(rv + j) * 2 + 1] * (acc[m][2][j] - s_stat[(rv + j) * 2] * cs);
        *reinterpret_cast<bf16x4*>(sVv + (wc * 16 + l15) * TS + rv * 2) = pack4(vv[0], vv[1], vv[2], vv[3]);
      }
    }
    __syncthreads();
#pragma unroll
    for (int i = 0; i < 2; ++i) {
      const int id = tid + i * 512;
      const int rr = tid >> 2, cc = tid & 3;
      const size_t hoff = ((size_t)(ct * 2 + i) * SEQ + row0 + rr) * 32 + cc * 8;
      st16(O0 + hoff, *reinterpret_cast<const u32x4*>(sQ + rr * QS + (i * 4 + cc) * 16));
      st16(O1 + hoff, *reinterpret_cast<const u32x4*>(sKk + rr * QS + (i * 4 + cc) * 16));
      const int e = id >> 4, c2 = id & 15;
      st16(O2 + (size_t)(ct * 64 + e) * SEQ + row0 + c2 * 8, *reinterpret_cast<const u32x4*>(sVv + e * TS + c2 * 16));
    }
  } else {
    char* pkT = smem + 128 * QS;
    char* vT = pkT + 64 * TS;
#pragma unroll
    for (int m = 0; m < 4; ++m) {
      const int rl = wr * 64 + m * 16 + l15;
      const float mean = s_stat[rl * 2], rstd = s_stat[rl * 2 + 1];
      {
        const float* cs = s_cs + wc * 16 + g * 4;
        float a0 = rbf(rstd * (acc[m][0][0] - mean * cs[0])), a1 = rbf(rstd * (acc[m][0][1] - mean * cs[1]));
        float a2 = rbf(rstd * (acc[m][0][2] - mean * cs[2])), a3 = rbf(rstd * (acc[m][0][3] - mean * cs[3]));
        *reinterpret_cast<bf16x4*>(sQ + rl * QS + (wc * 16 + g * 4) * 2) = pack4(phi_f(a0), phi_f(a1), phi_f(a2), phi_f(a3));
      }
      const int rv = wr * 64 + m * 16 + g * 4;
      const float csk = s_cs[64 + wc * 16 + l15], csv = s_cs[128 + wc * 16 + l15];
      float kk[4], vv[4];
#pragma unroll
      for (int j = 0; j < 4; ++j) {
        const float mn = s_stat[(rv + j) * 2], rs = s_stat[(rv + j) * 2 + 1];
        kk[j] = phi_f(rbf(rs * (acc[m][1][j] - mn * csk)));
        vv[j] = rs * (acc[m][2][j] - mn * csv);
      }
      *reinterpret_cast<bf16x4*>(pkT + (wc * 16 + l15) * TS + rv * 2) = pack4(kk[0], kk[1], kk[2], kk[3]);
      *reinterpret_cast<bf16x4*>(vT + (wc * 16 + l15) * TS + rv * 2) = pack4(vv[0], vv[1], vv[2], vv[3]);
    }
    __syncthreads();
    const int hh = wid >> 2, dt = (wid >> 1) & 1, et = wid & 1;
    f32x4 kv = {0.f, 0.f, 0.f, 0.f}, zz = {0.f, 0.f, 0.f, 0.f};
    bf16x8 ones;
#pragma unroll
    for (int j = 0; j < 8; ++j) ones[j] = (short)0x3F80;
#pragma unroll
    for (int ks = 0; ks < 4; ++ks) {
      bf16x8 a = *reinterpret_cast<const bf16x8*>(pkT + (hh * 32 + dt * 16 + l15) * TS + (ks * 32 + g * 8) * 2);
      bf16x8 b = *reinterpret_cast<const bf16x8*>(vT + (hh * 32 + et * 16 + l15) * TS + (ks * 32 + g * 8) * 2);
      kv = __builtin_amdgcn_mfma_f32_16x16x32_bf16(b, a, kv, 0, 0, 0);
      if (et == 0) zz = __builtin_amdgcn_mfma_f32_16x16x32_bf16(ones, a, zz, 0, 0, 0);
    }
    float* kp = kvpart + ((size_t)rb * NH + ct * 2 + hh) * (33 * 32);
    st16(kp + (dt * 16 + l15) * 32 + et * 16 + g * 4, __builtin_bit_cast(u32x4, kv));
    if (et == 0 && g == 0)
      __hip_atomic_store(kp + 32 * 32 + dt * 16 + l15, zz[0], __ATOMIC_RELAXED, __HIP_MEMORY_SCOPE_AGENT);
  }
}

__device__ __forceinline__ void linattn_phase(char* smem, const int rb, const int ct, const float* __restrict__ kvpart,
                                              u16* __restrict__ O) {
  constexpr int QS = 144;
  int tid = threadIdx.x;
  asm volatile("" : "+v"(tid));
  const int lane = tid & 63, wid = tid >> 6;
  const int g = lane >> 4, l15 = lane & 15;
  u16* kvT = reinterpret_cast<u16*>(smem + 64 * 1024);
  {
    const __amdgpu_buffer_rsrc_t rkv = mk_rsrc(kvpart, 16u * NH * 1056u * 4u);
    {
      f32x4 v[16];
#pragma unroll
      for (int r = 0; r < 16; ++r)
        v[r] = __builtin_bit_cast(f32x4, ld16(rkv, (unsigned)(((r * NH + ct * 2) * 1056 + tid * 4) * 4)));
      f32x4 acc = v[0];
#pragma unroll
      for (int r = 1; r < 16; ++r) acc += v[r];
      const int i = tid * 4;
      const int hh = i >= 1056 ? 1 : 0, ii = i - hh * 1056;
      const int d = ii >> 5, e = ii & 31;
#pragma unroll
      for (int j = 0; j < 4; ++j) {
        if (d < 32) kvT[(hh * 33 + e + j) * 40 + d] = f2bf(acc[j]); else kvT[(hh * 33 + 32) * 40 + e + j] = f2bf(acc[j]);
      }
    }
    if (tid < 16) {
      const int t2 = 512 + tid;
      f32x4 acc = {0.f, 0.f, 0.f, 0.f};
#pragma unroll
      for (int r = 0; r < 16; ++r)
        acc += __builtin_bit_cast(f32x4, ld16(rkv, (unsigned)(((r * NH + ct * 2) * 1056 + t2 * 4) * 4)));
      const int ii = t2 * 4 - 1056;
      const int d = ii >> 5, e = ii & 31;
#pragma unroll
      for (int j = 0; j < 4; ++j) {
        if (d < 32) kvT[(33 + e + j) * 40 + d] = f2bf(acc[j]); else kvT[(33 + 32) * 40 + e + j] = f2bf(acc[j]);
      }
    }
  }
  __syncthreads();
  const int hh = wid >> 2, wq = wid & 3;
  const u16* kb = kvT + hh * 33 * 40;
  bf16x8 b0 = *reinterpret_cast<const bf16x8*>(kb + l15 * 40 + g * 8);
  bf16x8 b1 = *reinterpret_cast<const bf16x8*>(kb + (16 + l15) * 40 + g * 8);
  bf16x8 bz = *reinterpret_cast<const bf16x8*>(kb + 32 * 40 + g * 8);
#pragma unroll
  for (int m = 0; m < 2; ++m) {
    const int rl = wq * 32 + m * 16 + l15;
    bf16x8 a = *reinterpret_cast<const bf16x8*>(smem + rl * QS + (hh * 32 + g * 8) * 2);
    f32x4 z4 = {0.f, 0.f, 0.f, 0.f};
    f32x4 n0 = __builtin_amdgcn_mfma_f32_16x16x32_bf16(b0, a, z4, 0, 0, 0);
    f32x4 n1 = __builtin_amdgcn_mfma_f32_16x16x32_bf16(b1, a, z4, 0, 0, 0);
    f32x4 dn = __builtin_amdgcn_mfma_f32_16x16x32_bf16(bz, a, z4, 0, 0, 0);
    const float den = rbf(rbf(dn[0]) + 1e-6f);
    const float inv = 1.f / den;
    char* so = smem + 96 * 1024 + rl * 144 + hh * 64;
    *reinterpret_cast<bf16x4*>(so + g * 8) = pack4(rbf(n0[0]) * inv, rbf(n0[1]) * inv, rbf(n0[2]) * inv, rbf(n0[3]) * inv);
    *reinterpret_cast<bf16x4*>(so + 32 + g * 8) = pack4(rbf(n1[0]) * inv, rbf(n1[1]) * inv, rbf(n1[2]) * inv, rbf(n1[3]) * inv);
  }
  __syncthreads();
#pragma unroll
  for (int i = 0; i < 2; ++i) {
    const int id = tid + i * 512, r = id >> 3, c = id & 7;
    st16(O + (size_t)(rb * 128 + r) * DM + ct * 64 + c * 8, *reinterpret_cast<const u32x4*>(smem + 96 * 1024 + r * 144 + c * 16));
  }
}

__device__ __forceinline__ void swa_phase(char* smem0, const int h0, const int qb, const u16* __restrict__ Q,
                                          const u16* __restrict__ K, const u16* __restrict__ Vt, u16* __restrict__ O) {
  constexpr int NK = 384;
  constexpr int VS = NK * 2 + 16;
  constexpr int HB = NK * 64 + 32 * VS;
  const int q0 = qb * 128, k0 = q0 - 256;
  int tid = threadIdx.x;
  asm volatile("" : "+v"(tid));
  const int lane = tid & 63, wid = tid >> 6;
  const int g = lane >> 4, l15 = lane & 15;
  const int qrow = q0 + wid * 16 + l15;
  bf16x8 qf2[2];
  {
    const __amdgpu_buffer_rsrc_t rq = mk_rsrc(Q, 4u << 20), rk = mk_rsrc(K, 4u << 20), rv = mk_rsrc(Vt, 4u << 20);
    bf16x8 v[2][3], w[2][3];
#pragma unroll
    for (int hh = 0; hh < 2; ++hh) {
      const int h = h0 + hh;
      qf2[hh] = __builtin_bit_cast(bf16x8, ld16(rq, (unsigned)(((h * SEQ + qrow) * 32 + g * 8) * 2)));
#pragma unroll
      for (int i = 0; i < 3; ++i) {
        const int id = tid + i * 512, key = id >> 2, c = id & 3;
        int kg = k0 + key; kg = kg < 0 ? 0 : kg;
        v[hh][i] = __builtin_bit_cast(bf16x8, ld16(rk, (unsigned)(((h * SEQ + kg) * 32 + c * 8) * 2)));
      }
#pragma unroll
      for (int i = 0; i < 3; ++i) {
        const int id = tid + i * 512, e = id / 48, c = id % 48;
        int kg = k0 + c * 8; kg = kg < 0 ? 0 : kg;
        w[hh][i] = __builtin_bit_cast(bf16x8, ld16(rv, (unsigned)(((h * 32 + e) * SEQ + kg) * 2)));
      }
    }
#pragma unroll
    for (int hh = 0; hh < 2; ++hh) {
      char* sK = smem0 + hh * HB;
      char* sV = sK + NK * 64;
#pragma unroll
      for (int i = 0; i < 3; ++i) {
        const int id = tid + i * 512, key = id >> 2, c = id & 3;
        *reinterpret_cast<bf16x8*>(sK + key * 64 + ((c ^ ((-(key >> 2)) & 3)) * 16)) = v[hh][i];
      }
#pragma unroll
      for (int i = 0; i < 3; ++i) {
        const int id = tid + i * 512, e = id / 48, c = id % 48;
        *reinterpret_cast<bf16x8*>(sV + e * VS + c * 16) = w[hh][i];
      }
    }
  }
  __syncthreads();
#pragma unroll
  for (int hh = 0; hh < 2; ++hh) {
  const char* sK = smem0 + hh * HB;
  const char* sV = sK + NK * 64;
  const bf16x8 qf = hh ? qf2[1] : qf2[0];
  f32x4 st[17];
  const int kchunk = (g ^ ((-(l15 >> 2)) & 3)) * 16;
  const int tfirst = (k0 + wid * 16 < 0) ? ((-(k0 + wid * 16)) >> 4) : 0;
#pragma unroll
  for (int t = 0; t < 17; ++t) {
    bf16x8 kf = *reinterpret_cast<const bf16x8*>(sK + ((wid + t) * 16 + l15) * 64 + kchunk);
    f32x4 z4 = {0.f, 0.f, 0.f, 0.f};
    st[t] = __builtin_amdgcn_mfma_f32_16x16x32_bf16(kf, qf, z4, 0, 0, 0);
  }
  const float NEG = -3.0e38f;
#pragma unroll
  for (int j = 0; j < 4; ++j) {
    st[0][j] = (g * 4 + j > l15) ? st[0][j] : NEG;
    st[16][j] = (g * 4 + j <= l15) ? st[16][j] : NEG;
  }
  if (tfirst > 0) {
#pragma unroll
    for (int t = 0; t < 16; ++t)
      if (t < tfirst) st[t] = (f32x4){NEG, NEG, NEG, NEG};
  }
  float mx = NEG;
#pragma unroll
  for (int t = 0; t < 17; ++t)
    mx = fmaxf(mx, fmaxf(fmaxf(st[t][0], st[t][1]), fmaxf(st[t][2], st[t][3])));
  mx = fmaxf(mx, __shfl_xor(mx, 16));
  mx = fmaxf(mx, __shfl_xor(mx, 32));
  const float c = 0.17677669529663687f * 1.4426950408889634f;
  const float mc = mx * c;
  unsigned pk[18][2];
#pragma unroll
  for (int t = 0; t < 17; ++t) {
    float p0 = __builtin_amdgcn_exp2f(st[t][0] * c - mc), p1 = __builtin_amdgcn_exp2f(st[t][1] * c - mc);
    float p2 = __builtin_amdgcn_exp2f(st[t][2] * c - mc), p3 = __builtin_amdgcn_exp2f(st[t][3] * c - mc);
    pk[t][0] = cvt_pk_bf16(p0, p1);
    pk[t][1] = cvt_pk_bf16(p2, p3);
  }
  pk[17][0] = 0u; pk[17][1] = 0u;
  f32x4 o0 = {0.f, 0.f, 0.f, 0.f}, o1 = {0.f, 0.f, 0.f, 0.f}, os = {0.f, 0.f, 0.f, 0.f};
  bf16x8 ones;
#pragma unroll
  for (int j = 0; j < 8; ++j) ones[j] = (short)0x3F80;
#pragma unroll
  for (int u = 0; u < 9; ++u) {
    const int t0 = 2 * u, t1 = 2 * u + 1;
    u32x4 pw = {pk[t0][0], pk[t0][1], pk[t1][0], pk[t1][1]};
    bf16x8 pf = __builtin_bit_cast(bf16x8, pw);
    const int ka = (wid + t0) * 16 + g * 4;
    const int kb = (u < 8) ? (wid + t1) * 16 + g * 4 : ka;
    u32x2 va0 = *reinterpret_cast<const u32x2*>(sV + l15 * VS + ka * 2);
    u32x2 vb0 = *reinterpret_cast<const u32x2*>(sV + l15 * VS + kb * 2);
    u32x2 va1 = *reinterpret_cast<const u32x2*>(sV + (16 + l15) * VS + ka * 2);
    u32x2 vb1 = *reinterpret_cast<const u32x2*>(sV + (16 + l15) * VS + kb * 2);
    u32x4 v0 = {va0[0], va0[1], vb0[0], vb0[1]};
    u32x4 v1 = {va1[0], va1[1], vb1[0], vb1[1]};
    o0 = __builtin_amdgcn_mfma_f32_16x16x32_bf16(__builtin_bit_cast(bf16x8, v0), pf, o0, 0, 0, 0);
    o1 = __builtin_amdgcn_mfma_f32_16x16x32_bf16(__builtin_bit_cast(bf16x8, v1), pf, o1, 0, 0, 0);
    os = __builtin_amdgcn_mfma_f32_16x16x32_bf16(ones, pf, os, 0, 0, 0);
  }
  const float inv = 1.f / os[0];
  u32x2 r0 = {cvt_pk_bf16(o0[0] * inv, o0[1] * inv), cvt_pk_bf16(o0[2] * inv, o0[3] * inv)};
  u32x2 r1 = {cvt_pk_bf16(o1[0] * inv, o1[1] * inv), cvt_pk_bf16(o1[2] * inv, o1[3] * inv)};
  {
    char* so = smem0 + 2 * HB + (wid * 16 + l15) * 144 + hh * 64;
    *reinterpret_cast<u32x2*>(so + g * 8) = r0;
    *reinterpret_cast<u32x2*>(so + 32 + g * 8) = r1;
  }
  }
  __syncthreads();
#pragma unroll
  for (int i = 0; i < 2; ++i) {
    const int id = tid + i * 512, r = id >> 3, c = id & 7;
    st16(O + (size_t)(q0 + r) * DM + h0 * 32 + c * 8, *reinterpret_cast<const u32x4*>(smem0 + 2 * HB + r * 144 + c * 16));
  }
}

constexpr int OP_STAGE = 16384 + 8192;
__device__ __forceinline__ void oproj_phase(char* smem, const int rb, const int ct, const u16* __restrict__ A,
                                            const u16* __restrict__ W, const u16* __restrict__ Xin,
                                            u16* __restrict__ Xout, float* __restrict__ statp, unsigned* wait_cnt) {
  int tid = threadIdx.x;
  asm volatile("" : "+v"(tid));
  const int lane = tid & 63, wid = tid >> 6;
  const int wr = wid >> 1, wc = wid & 1;
  const int g = lane >> 4, l15 = lane & 15;
  const int row0 = rb * 128, col0 = ct * 64;
  const u16* srcA0; const u16* srcA1; const u16* srcB;
  {
    const int r0 = (2 * wid) * 8 + (lane >> 3), r1 = r0 + 8;
    const int c = lane & 7;
    srcA0 = A + (size_t)(row0 + r0) * DM + ((c ^ (r0 & 7)) * 8);
    srcA1 = A + (size_t)(row0 + r1) * DM + ((c ^ (r1 & 7)) * 8);
    const int kb = wid;
    const int rowp = l15 >> 1, half = lane & 1, nb4 = lane >> 4;
    const int k = kb * 8 + (rowp ^ ((kb & 1) * 4));
    srcB = W + (size_t)k * DM + col0 + nb4 * 16 + half * 8;
  }
  auto stage_piece = [&](int kt, int buf, const int i) {
    char* base = smem + buf * OP_STAGE;
    if (i == 0) __builtin_amdgcn_global_load_lds((const unsigned*)(srcA0 + kt * 64), (LDS_AS unsigned*)(base + (2 * wid) * 1024), 16, 0, 16);
    else if (i == 1) __builtin_amdgcn_global_load_lds((const unsigned*)(srcA1 + kt * 64), (LDS_AS unsigned*)(base + (2 * wid + 1) * 1024), 16, 0, 16);
    else __builtin_amdgcn_global_load_lds((const unsigned*)(srcB + (size_t)kt * 64 * DM), (LDS_AS unsigned*)(base + 16384 + wid * 1024), 16, 0, 0);
  };
#pragma unroll
  for (int s2 = 0; s2 < 6; ++s2) stage_piece(s2, s2, 2);
  if (threadIdx.x == 0) poll_ge(wait_cnt, 16u);
  asm volatile("s_waitcnt lgkmcnt(0)" ::: "memory");
  BAR();
  u32x4 xr0, xr1;
  {
    const u16* xp0 = Xin + (size_t)(row0 + (tid >> 3)) * DM + col0 + (tid & 7) * 8;
    const u16* xp1 = xp0 + (size_t)64 * DM;
    asm volatile("global_load_dwordx4 %0, %2, off sc1\n\tglobal_load_dwordx4 %1, %3, off sc1"
                 : "=&v"(xr0), "=&v"(xr1) : "v"(xp0), "v"(xp1) : "memory");
  }
#pragma unroll
  for (int s2 = 0; s2 < 6; ++s2) {
    stage_piece(s2, s2, 0);
    stage_piece(s2, s2, 1);
  }
  f32x4 acc[2][2];
#pragma unroll
  for (int m = 0; m < 2; ++m)
#pragma unroll
    for (int n = 0; n < 2; ++n) acc[m][n] = (f32x4){0.f, 0.f, 0.f, 0.f};
  const int r7 = l15 & 7;
  const int q4 = l15 >> 2, p4 = lane & 3;
  const unsigned sbase = (unsigned)(size_t)smem;
  const unsigned aoff0 = sbase + (wr * 32 + l15) * 128 + ((g ^ r7) * 16);
  const unsigned aoff1 = sbase + (wr * 32 + l15) * 128 + (((4 + g) ^ r7) * 16);
  const int sw = (g & 1) * 4;
  const unsigned boff0 = sbase + 16384 + g * 1024 + wc * 512 + p4 * 8 + ((q4 ^ sw) * 32);
  const unsigned boff1 = sbase + 16384 + g * 1024 + wc * 512 + p4 * 8 + (((4 + q4) ^ sw) * 32);
  constexpr int NT = DM / 64;
  bf16x8 af[2][2][2];
  bf16x4 blo[2][2][2], bhi[2][2][2];
  auto read_all = [&](int kt, const int S) {
    const unsigned bo = (kt % 6) * OP_STAGE;
    const unsigned a0 = aoff0 + bo, a1 = aoff1 + bo, b0 = boff0 + bo, b1 = boff1 + bo;
    SCHED();
    DS_TR64(blo[S][0][0], b0, 0);    DS_TR64(bhi[S][0][0], b1, 0);
    DS_B128(af[S][0][0], a0, 0);
    DS_TR64(blo[S][0][1], b0, 256);  DS_TR64(bhi[S][0][1], b1, 256);
    DS_B128(af[S][0][1], a0, 2048);
    DS_TR64(blo[S][1][0], b0, 4096); DS_TR64(bhi[S][1][0], b1, 4096);
    DS_B128(af[S][1][0], a1, 0);
    DS_TR64(blo[S][1][1], b0, 4352); DS_TR64(bhi[S][1][1], b1, 4352);
    DS_B128(af[S][1][1], a1, 2048);
    SCHED();
  };
  auto mma = [&](const int S, int ktn, bool do_stage) {
#pragma unroll
    for (int ks = 0; ks < 2; ++ks) {
      SCHED();
      bf16x8 bfr[2];
#pragma unroll
      for (int n = 0; n < 2; ++n) bfr[n] = __builtin_shufflevector(blo[S][ks][n], bhi[S][ks][n], 0, 1, 2, 3, 4, 5, 6, 7);
#pragma unroll
      for (int m = 0; m < 2; ++m) {
#pragma unroll
        for (int n = 0; n < 2; ++n)
          acc[m][n] = __builtin_amdgcn_mfma_f32_16x16x32_bf16(bfr[n], af[S][ks][m], acc[m][n], 0, 0, 0);
        SCHED();
        if (do_stage && ks * 2 + m < 3) stage_piece(ktn, ktn % 6, ks * 2 + m);
        SCHED();
      }
    }
  };
  WAIT_V(10);
  BAR();
  read_all(0, 0);
#pragma unroll
  for (int kt = 0; kt < NT; ++kt) {
    if (kt == 0) { WAIT_V(8); } else if (kt == 1) { WAIT_V(9); } else if (kt == 2) { WAIT_V(10); } else if (kt == 3) { WAIT_V(11); }
    else if (kt + 5 < NT) { WAIT_V(12); } else if (kt + 4 < NT) { WAIT_V(9); } else if (kt + 3 < NT) { WAIT_V(6); }
    else if (kt + 2 < NT) { WAIT_V(3); } else { WAIT_V(0); }
    WAIT_L(0);
    BAR();
    if (kt + 1 < NT) read_all(kt + 1, (kt + 1) & 1);
    mma(kt & 1, kt + 6, kt + 6 < NT);
  }
  constexpr int FS = 272;
  BAR();
  asm volatile("s_waitcnt vmcnt(0)" : "+v"(xr0), "+v"(xr1) :: "memory");
#pragma unroll
  for (int m = 0; m < 2; ++m)
#pragma unroll
    for (int n = 0; n < 2; ++n)
      *reinterpret_cast<f32x4*>(smem + (wr * 32 + m * 16 + l15) * FS + (wc * 32 + n * 16 + g * 4) * 4) = acc[m][n];
  __syncthreads();
#pragma unroll
  for (int i = 0; i < 2; ++i) {
    const int id = tid + i * 512;
    const int r = id >> 3, c = id & 7;
    const int row = row0 + r, col = col0 + c * 8;
    const bf16x8 xi = __builtin_bit_cast(bf16x8, i == 0 ? xr0 : xr1);
    f32x4 a0 = *reinterpret_cast<const f32x4*>(smem + r * FS + c * 32);
    f32x4 a1 = *reinterpret_cast<const f32x4*>(smem + r * FS + c * 32 + 16);
    float y[8];
    float sm = 0.f, q = 0.f;
#pragma unroll
    for (int j = 0; j < 4; ++j) {
      y[j] = rbf(bf2f((u16)xi[j]) + rbf(a0[j]));
      y[4 + j] = rbf(bf2f((u16)xi[4 + j]) + rbf(a1[j]));
    }
#pragma unroll
    for (int j = 0; j < 8; ++j) { sm += y[j]; q += y[j] * y[j]; }
    u32x4 yo = {cvt_pk_bf16(y[0], y[1]), cvt_pk_bf16(y[2], y[3]), cvt_pk_bf16(y[4], y[5]), cvt_pk_bf16(y[6], y[7])};
    st16(Xout + (size_t)row * DM + col, yo);
    sm += __shfl_xor(sm, 1); q += __shfl_xor(q, 1);
    sm += __shfl_xor(sm, 2); q += __shfl_xor(q, 2);
    sm += __shfl_xor(sm, 4); q += __shfl_xor(q, 4);
    if (c == 0) {
      f32x2 o; o[0] = sm; o[1] = q;
      st8(statp + ((size_t)row * 16 + ct) * 2, __builtin_bit_cast(u32x2, o));
    }
  }
}

template <int L>
__device__ __forceinline__ void layer(char* smem, const int rb, const int ct, const u16* __restrict__ x0,
                                      const u16* __restrict__ Wqkv, const u16* __restrict__ Wo,
                                      u16* __restrict__ out, char* __restrict__ ws) {
  unsigned* cnt = reinterpret_cast<unsigned*>(ws + OFF_CNT);
  const float* colsum = reinterpret_cast<const float*>(ws + OFF_COLSUM) + (L & 1) * 3072;
  const u16* xin = L == 0 ? x0 : reinterpret_cast<const u16*>(ws + off_x(L > 0 ? L - 1 : 0));
  u16* xout = L == 3 ? out : reinterpret_cast<u16*>(ws + off_x(L));
  u16* bq = reinterpret_cast<u16*>(ws + off_q(L));
  u16* bk = reinterpret_cast<u16*>(ws + off_k(L));
  u16* bv = reinterpret_cast<u16*>(ws + off_v(L));
  u16* bo = reinterpret_cast<u16*>(ws + off_o(L));
  const float* stin = reinterpret_cast<const float*>(ws + off_stat(L));
  float* stout = reinterpret_cast<float*>(ws + off_stat(L + 1));
  float* kvp = reinterpret_cast<float*>(ws + off_kv(L));
  unsigned* wx = L > 0 ? cnt + cidx_x(L, rb) : nullptr;
  if ((L & 1) == 0) {
    qkv_phase<0>(smem, rb, ct, xin, Wqkv, colsum, stin, bq, bk, bv, kvp, wx);
    signal_add(cnt + cidx_kv(L, ct));
    if (threadIdx.x == 0) poll_ge(cnt + cidx_kv(L, ct), 16u);
    __syncthreads();
    linattn_phase(smem, rb, ct, kvp, bo);
  } else {
    qkv_phase<1>(smem, rb, ct, xin, Wqkv, colsum, stin, bq, bk, bv, kvp, wx);
    signal_add(cnt + cidx_q(L, rb, ct));
    if (threadIdx.x == 0) {
      if (rb >= 1) poll_ge(cnt + cidx_q(L, rb - 1, ct), 1u);
      if (rb >= 2) poll_ge(cnt + cidx_q(L, rb - 2, ct), 1u);
    }
    __syncthreads();
    swa_phase(smem, ct * 2, rb, bq, bk, bv, bo);
  }
  signal_add(cnt + cidx_o(L, rb));
  oproj_phase(smem, rb, ct, bo, Wo, xin, xout, stout, cnt + cidx_o(L, rb));
  if (L < 3) signal_add(cnt + cidx_x(L + 1, rb));
}

__global__ __launch_bounds__(512, 2) void mega_kernel(const u16* __restrict__ x0, const u16* __restrict__ WqkvA,
                                                      const u16* __restrict__ WoA, const u16* __restrict__ WqkvB,
                                                      const u16* __restrict__ WoB, u16* __restrict__ out,
                                                      char* __restrict__ ws) {
  __shared__ __attribute__((aligned(16))) char smem[SMEM_BYTES];
  const int bid = blockIdx.x;
  const int xcd = bid & 7, idx = bid >> 3;
  const int rb = (xcd >> 2) * 8 + (idx & 7);
  const int ct = (xcd & 3) * 4 + (idx >> 3);
  layer<0>(smem, rb, ct, x0, WqkvA, WoA, out, ws);
  layer<1>(smem, rb, ct, x0, WqkvB, WoB, out, ws);
  layer<2>(smem, rb, ct, x0, WqkvA, WoA, out, ws);
  layer<3>(smem, rb, ct, x0, WqkvB, WoB, out, ws);
}

extern "C" void kernel_launch(void* const* d_in, const int* in_sizes, int n_in, void* d_out, int out_size,
                              void* d_ws, size_t ws_size, hipStream_t stream) {
  const u16* x0 = (const u16*)d_in[0];
  const u16* A_wqkv = (const u16*)d_in[1];
  const u16* A_wo = (const u16*)d_in[2];
  const u16* B_wqkv = (const u16*)d_in[4];
  const u16* B_wo = (const u16*)d_in[5];
  char* ws = (char*)d_ws;
  prep_kernel<<<96 + 512 + 64, 256, 0, stream>>>(A_wqkv, B_wqkv, x0, (float*)(ws + OFF_COLSUM), (float*)(ws + off_stat(0)),
                                                 A_wo, B_wo, (float*)(ws + OFF_SINK), (unsigned*)(ws + OFF_CNT));
  mega_kernel<<<256, 512, 0, stream>>>(x0, A_wqkv, A_wo, B_wqkv, B_wo, (u16*)d_out, ws);
}
```
